```python
import torch

def _softmax(x, axis=-1):
    return torch.softmax(x, dim=axis)
def _gelu(x):
    return torch.nn.functional.gelu(x, approximate="tanh")
def _silu(x):
    return torch.nn.functional.silu(x)
def _sigmoid(x):
    return torch.sigmoid(x)
def _layernorm(x, axis=-1, eps=1e-5):
    m = x.mean(dim=axis, keepdim=True)
    v = x.var(dim=axis, keepdim=True, unbiased=False)
    return (x - m) / torch.sqrt(v + eps)
def _scatter_add(tgt, idx, src, axis=0):
    return tgt.clone().index_add_(axis, idx, src)
def _segment_sum(src, seg, n):
    out = torch.zeros((n,) + src.shape[1:], dtype=src.dtype)
    return out.index_add_(0, seg, src)
def _pad(x, pw):
    flat = tuple(v for pair in reversed(pw) for v in pair)
    return torch.nn.functional.pad(x, flat)

kernel_name = 'recurrent_scan__gqa_block_cached__dense_activation_relu__decode'

def setup_inputs():

    _w = (lambda a: (lambda t: t.to(torch.bfloat16) if t.is_floating_point() else t)(torch.as_tensor(a)))
    def _A_setup():
        B, Sq, D = 8, 1, 448
        Hq, Hkv, Sk, Dh = 16, 8, 4096, 256
        assert Hq % Hkv == 0, (Hq, Hkv)
        return {
            "x": _w((lambda *s: torch.randn(*s, dtype=torch.bfloat16) * 0.05)(B, Sq, D)),
            "k_cache": _w((lambda *s: torch.randn(*s, dtype=torch.bfloat16) * 0.8)(B, Hkv, Sk, Dh)),
            "v_cache": _w((lambda *s: torch.randn(*s, dtype=torch.bfloat16) * 0.8)(B, Hkv, Sk, Dh)),
            "wq": _w((lambda *s: torch.randn(*s, dtype=torch.bfloat16) * 0.8)(D, Hq * Dh) * (D ** -0.5)),
            "wo": _w((lambda *s: torch.randn(*s, dtype=torch.bfloat16) * 0.8)(Hq * Dh, D) * ((Hq * Dh) ** -0.5)),
        }
    _A_in = _A_setup()

    def _B_setup():
        B, S, D, Dff = 8, 1, 448, 1792
        return {
            "x": _w((lambda *s: torch.randn(*s, dtype=torch.bfloat16) * 0.05)(B, S, D)),
            "w1": _w((lambda *s: torch.randn(*s, dtype=torch.bfloat16) * 0.2)(D, Dff)),
            "w2": _w((lambda *s: torch.randn(*s, dtype=torch.bfloat16) * 0.2)(Dff, D)),
        }
    _B_in = _B_setup()

    return {
        "A_x": _A_in["x"],
        "A_k_cache": _A_in["k_cache"],
        "A_v_cache": _A_in["v_cache"],
        "A_wq": _A_in["wq"],
        "A_wo": _A_in["wo"],
        "B_x": _B_in["x"],
        "B_w1": _B_in["w1"],
        "B_w2": _B_in["w2"],
    }

def reference(A_x, A_k_cache, A_v_cache, A_wq, A_wo, B_x, B_w1, B_w2):
    def _A_ref(x, k_cache, v_cache, wq, wo):
        B, Sq, D = x.shape
        Hq, Dh = 16, 256
        Hkv = k_cache.shape[1]
        rep = Hq // Hkv
        q = (lambda x, *d: x.permute(*d))((x @ wq).reshape(B, Sq, Hq, Dh), 0, 2, 1, 3)
        k = (lambda x, n, axis: torch.repeat_interleave(x, n, dim=axis))(k_cache, rep, axis=1)
        v = (lambda x, n, axis: torch.repeat_interleave(x, n, dim=axis))(v_cache, rep, axis=1)
        scores = torch.einsum("bhqd,bhkd->bhqk", q, k) * (Dh ** -0.5)
        p = _softmax(scores, axis=-1)
        o = torch.einsum("bhqk,bhkd->bhqd", p, v)
        return (lambda x, *d: x.permute(*d))(o, 0, 2, 1, 3).reshape(B, Sq, Hq * Dh) @ wo

    def _B_ref(x, w1, w2):
        h = torch.einsum("bsd,df->bsf", x, w1)
        zero = torch.zeros_like(h)
        h = torch.maximum(h, zero)
        return torch.einsum("bsf,fd->bsd", h, w2)

    x = A_x
    xn = _layernorm(x, axis=-1)
    s = _A_ref(xn, A_k_cache, A_v_cache, A_wq, A_wo)
    g = _silu(_B_ref(xn, B_w1, B_w2))
    return x + s * g
```

```cpp
#include <hip/hip_runtime.h>
#include <hip/hip_bf16.h>
#include <cstdint>

namespace {

typedef unsigned u32x4 __attribute__((ext_vector_type(4)));
typedef unsigned u32x2 __attribute__((ext_vector_type(2)));
typedef float f32x4 __attribute__((ext_vector_type(4)));
typedef float f32x2 __attribute__((ext_vector_type(2)));
typedef short bf16x8 __attribute__((ext_vector_type(8)));
typedef __bf16 bf16x2 __attribute__((ext_vector_type(2)));
#define LAS __attribute__((address_space(3)))
typedef __amdgpu_buffer_rsrc_t rsrc_t;

constexpr int NB = 8, DM = 448, HKV = 8, SK = 4096, DH = 256, DFF = 1792, HD = 4096;
constexpr int NWG = 256, NTHR = 512;
constexpr int AUX_NT = 2, AUX_SC1 = 16, RSRC_W3 = 0x00020000;
#define WAUX AUX_NT
constexpr int CAP = 32;
#define PACE 8
constexpr int CHUNK = 512 * 1024;
constexpr unsigned SPIN_CAP = 1u << 18;

constexpr int WS_EPOCH = 0;
constexpr int WS_QG = 4096;
constexpr int WS_S1 = WS_QG + NB * HKV * 128 * 16;
constexpr int WS_ML = WS_S1 + NB * HKV * 4 * 256 * 16;
constexpr int WS_S2 = WS_ML + NB * HKV * 8 * 16;
constexpr int WS_GG = WS_S2 + NB * 32 * 224 * 16;
constexpr int WS_END = WS_GG + 28 * NB * 224 * 16;
constexpr int WS_TOTAL = WS_END;

constexpr int XN_STRIDE = 912;
constexpr int L_XN = 0;
constexpr int L_TAG = L_XN + 8 * XN_STRIDE;
constexpr int L_Q = L_TAG + 16;
constexpr int L_P = L_Q + 1024;
constexpr int L_MRG = L_P + 4 * 2048;
constexpr int L_ML = L_MRG + 4 * 2048;
constexpr int L_F = L_ML + 64;
constexpr int L_S = L_F + 4096;
constexpr int L_END = L_S + 4 * 448 * 4;
constexpr int LDS_BYTES = 96 * 1024;
static_assert(L_END <= LDS_BYTES, "lds");

struct Params {
  const void* x; const void* k; const void* v; const void* wq; const void* wo; const void* w1; const void* w2;
  void* out; void* ws;
};

__device__ __forceinline__ rsrc_t mk_rsrc(const void* p, int bytes) {
  return __builtin_amdgcn_make_buffer_rsrc(const_cast<void*>(p), 0, bytes, RSRC_W3);
}
__device__ __forceinline__ float asf(unsigned u) { return __builtin_bit_cast(float, u); }
__device__ __forceinline__ unsigned asu(float f) { return __builtin_bit_cast(unsigned, f); }
__device__ __forceinline__ float bf_lo(unsigned u) { return asf(u << 16); }
__device__ __forceinline__ float bf_hi(unsigned u) { return asf(u & 0xffff0000u); }
__device__ __forceinline__ unsigned pack_bf16(float lo, float hi) {
  bf16x2 r; r[0] = (__bf16)lo; r[1] = (__bf16)hi;
  return __builtin_bit_cast(unsigned, r);
}
__device__ __forceinline__ float rnd_bf16(float f) { return (float)(__bf16)f; }
__device__ __forceinline__ float dot2(unsigned a, unsigned b, float c) {
  return __builtin_amdgcn_fdot2_f32_bf16(__builtin_bit_cast(bf16x2, a), __builtin_bit_cast(bf16x2, b), c, false);
}
__device__ __forceinline__ unsigned perm_lo(unsigned a, unsigned b) { return __builtin_amdgcn_perm(b, a, 0x05040100u); }
__device__ __forceinline__ unsigned perm_hi(unsigned a, unsigned b) { return __builtin_amdgcn_perm(b, a, 0x07060302u); }
template <int CTRL> __device__ __forceinline__ float dpp(float x) {
  return asf((unsigned)__builtin_amdgcn_mov_dpp((int)asu(x), CTRL, 0xf, 0xf, true));
}
constexpr int XOR1 = 0xB1, XOR2 = 0x4E, XOR7 = 0x141, XOR8 = 0x128;
__device__ __forceinline__ void swap16(float& a, float& b) {
  auto s = __builtin_amdgcn_permlane16_swap(asu(a), asu(b), false, false);
  a = asf(s[0]); b = asf(s[1]);
}
__device__ __forceinline__ void swap32(float& a, float& b) {
  auto s = __builtin_amdgcn_permlane32_swap(asu(a), asu(b), false, false);
  a = asf(s[0]); b = asf(s[1]);
}
__device__ __forceinline__ float x16_sum(float x) { float a = x, b = x; swap16(a, b); return a + b; }
__device__ __forceinline__ float x32_sum(float x) { float a = x, b = x; swap32(a, b); return a + b; }
__device__ __forceinline__ float x16_max(float x) { float a = x, b = x; swap16(a, b); return fmaxf(a, b); }
__device__ __forceinline__ float x32_max(float x) { float a = x, b = x; swap32(a, b); return fmaxf(a, b); }
__device__ __forceinline__ float row_sum(float x) { x += dpp<XOR1>(x); x += dpp<XOR2>(x); x += dpp<XOR7>(x); x += dpp<XOR8>(x); return x; }
__device__ __forceinline__ float row_max(float x) {
  x = fmaxf(x, dpp<XOR1>(x)); x = fmaxf(x, dpp<XOR2>(x)); x = fmaxf(x, dpp<XOR7>(x)); x = fmaxf(x, dpp<XOR8>(x)); return x;
}
__device__ __forceinline__ float wave_sum(float x) { return x32_sum(x16_sum(row_sum(x))); }
__device__ __forceinline__ f32x4 mfma16(u32x4 a, u32x4 b, f32x4 c) {
  return __builtin_amdgcn_mfma_f32_16x16x32_bf16(__builtin_bit_cast(bf16x8, a), __builtin_bit_cast(bf16x8, b), c, 0, 0, 0);
}
__device__ __forceinline__ void wg_barrier() { asm volatile("s_waitcnt lgkmcnt(0)\n\ts_barrier" ::: "memory"); }
__device__ __forceinline__ void lds_fence() { asm volatile("s_waitcnt lgkmcnt(0)" ::: "memory"); }
#define SCHED() __builtin_amdgcn_sched_barrier(0)

__device__ __forceinline__ void st_gran(rsrc_t ws, int off, unsigned v0, unsigned v1, unsigned tag) {
  __builtin_amdgcn_raw_buffer_store_b128(u32x4{v0, tag, v1, ~tag}, ws, off, 0, AUX_SC1);
}
__device__ __forceinline__ u32x4 ld_gran(rsrc_t ws, int off) { return __builtin_amdgcn_raw_buffer_load_b128(ws, off, 0, AUX_SC1); }
__device__ __forceinline__ bool gran_ok(u32x4 g, unsigned tag) { return g[1] == tag && g[3] == ~tag; }
template <int N> __device__ __forceinline__ void sweep(rsrc_t ws, const int (&off)[N], unsigned tag, u32x4 (&g)[N]) {
#pragma unroll 1
  for (unsigned spins = 0; spins < SPIN_CAP; ++spins) {
    asm volatile("" ::: "memory");
    bool ok = true;
#pragma unroll
    for (int i = 0; i < N; ++i) g[i] = ld_gran(ws, off[i]);
#pragma unroll
    for (int i = 0; i < N; ++i) ok &= gran_ok(g[i], tag);
    if (__all(ok)) break;
    __builtin_amdgcn_s_sleep(1);
  }
}
#define STAMP(i) do {} while (0)

template <int NW> __device__ __forceinline__ u32x4 frag_col(const unsigned (&r)[8][NW], int m) {
  u32x4 f;
#pragma unroll
  for (int i = 0; i < 4; ++i)
    f[i] = (m & 1) ? perm_hi(r[2 * i][m >> 1], r[2 * i + 1][m >> 1]) : perm_lo(r[2 * i][m >> 1], r[2 * i + 1][m >> 1]);
  return f;
}

__global__ void __launch_bounds__(NTHR, 2) fused_decode_kernel(Params p) {
  extern __shared__ __attribute__((aligned(16))) unsigned char smem[];
  LAS unsigned char* L = (LAS unsigned char*)smem;
  const int tid = threadIdx.x, lane = tid & 63, w = __builtin_amdgcn_readfirstlane(tid >> 6);
  const int blk = blockIdx.x, kvh = blk & 7, i32 = blk >> 3, j = i32 & 3, b = i32 >> 2;
  const rsrc_t ws = mk_rsrc(p.ws, WS_TOTAL);
  const int c16 = lane & 15, g4 = lane >> 4, sub = c16 >> 3;
  STAMP(0);

  const rsrc_t rx = mk_rsrc(p.x, NB * DM * 2);
  const u32x4 xr = __builtin_amdgcn_raw_buffer_load_b128(rx, lane < 56 ? w * (DM * 2) + lane * 16 : 0x7fffff00, 0, 0);

  const size_t kv_off = ((size_t)(b * HKV + kvh) * 4 + j) * CHUNK;
  const rsrc_t rk = mk_rsrc((const char*)p.k + kv_off, CHUNK);
  const rsrc_t rv = mk_rsrc((const char*)p.v + kv_off, CHUNK);

  auto layernorm = [&]() {
    float xv[8];
#pragma unroll
    for (int i = 0; i < 4; ++i) { xv[2 * i] = bf_lo(xr[i]); xv[2 * i + 1] = bf_hi(xr[i]); }
    float s = ((xv[0] + xv[1]) + (xv[2] + xv[3])) + ((xv[4] + xv[5]) + (xv[6] + xv[7]));
    const float mean = wave_sum(s) * (1.0f / DM);
    float ss = 0.f;
#pragma unroll
    for (int i = 0; i < 8; ++i) { const float d = xv[i] - mean; ss = fmaf(d, d, ss); }
    if (lane >= 56) ss = 0.f;
    const float var = rnd_bf16(wave_sum(ss) * (1.0f / DM));
    const float mb = rnd_bf16(mean);
    const float den = rnd_bf16(__builtin_sqrtf(rnd_bf16(var + 1e-5f)));
    const float rden = __builtin_amdgcn_rcpf(den);
    u32x4 o;
#pragma unroll
    for (int i = 0; i < 4; ++i) o[i] = pack_bf16(rnd_bf16(xv[2 * i] - mb) * rden, rnd_bf16(xv[2 * i + 1] - mb) * rden);
    if (lane < 56) *(LAS u32x4*)(L + L_XN + w * XN_STRIDE + lane * 16) = o;
  };

  if (w < 4) {
    const int voff = w * 1024 + lane * 16;
    u32x4 ring[CAP];
#pragma unroll
    for (int i = 0; i < PACE; ++i) ring[i] = __builtin_amdgcn_raw_buffer_load_b128(rk, voff, i * 4096, AUX_NT);
    SCHED();
    layernorm();
    wg_barrier();
    STAMP(1);
#pragma unroll
    for (int i = PACE; i < CAP; ++i) {
      asm volatile("s_waitcnt vmcnt(%0)" ::"n"(PACE - 1) : "memory");
      ring[i] = __builtin_amdgcn_raw_buffer_load_b128(rk, voff, i * 4096, AUX_NT);
      SCHED();
    }
    STAMP(2);
    wg_barrier();
    STAMP(3);
    const int hf = lane >> 5;
    u32x4 q0 = *(LAS u32x4*)(L + L_Q + (lane & 31) * 16), q1 = *(LAS u32x4*)(L + L_Q + 512 + (lane & 31) * 16);
    LAS float* pw = (LAS float*)(L + L_P + w * 2048);
    const int islot = ((lane >> 3) & 1) | ((lane >> 1) & 2) | ((lane << 1) & 4) | ((lane << 3) & 8);
    const int pidx = islot * 4 + hf * 2 + (g4 & 1);
    constexpr float C2 = 0.0625f * 1.4426950408889634f;
    float mx = -INFINITY;
#pragma unroll 1
    for (int it = 0; it < 4; ++it) {
      const rsrc_t rn = it < 3 ? rk : rv;
      const int sbase = it < 3 ? (it + 1) * (CAP * 4096) : 0;
      float l1[2], l2[2], l3[2], l4[2];
#pragma unroll
      for (int i = 0; i < CAP; ++i) {
        const u32x4 kk = ring[i];
        float a = dot2(kk[0], q0[0], 0.f), c = dot2(kk[0], q1[0], 0.f);
#pragma unroll
        for (int e = 1; e < 4; ++e) { a = dot2(kk[e], q0[e], a); c = dot2(kk[e], q1[e], c); }
        ring[i] = __builtin_amdgcn_raw_buffer_load_b128(rn, voff, sbase + i * 4096, AUX_NT);
        swap16(a, c);
        l1[i & 1] = a + c;
        if ((i & 1) == 1) {
          const float t1 = l1[0] + dpp<XOR8>(l1[0]), t2 = l1[1] + dpp<XOR8>(l1[1]);
          l2[(i >> 1) & 1] = (lane & 8) ? t2 : t1;
        }
        if ((i & 3) == 3) {
          const float t1 = l2[0] + dpp<XOR7>(l2[0]), t2 = l2[1] + dpp<XOR7>(l2[1]);
          l3[(i >> 2) & 1] = (lane & 4) ? t2 : t1;
        }
        if ((i & 7) == 7) {
          const float t1 = l3[0] + dpp<XOR2>(l3[0]), t2 = l3[1] + dpp<XOR2>(l3[1]);
          l4[(i >> 3) & 1] = (lane & 2) ? t2 : t1;
        }
        if ((i & 15) == 15) {
          const float t1 = l4[0] + dpp<XOR1>(l4[0]), t2 = l4[1] + dpp<XOR1>(l4[1]);
          const float sc = rnd_bf16((lane & 1) ? t2 : t1) * C2;
          mx = fmaxf(mx, sc);
          pw[(it * CAP + (i - 15)) * 4 + pidx] = sc;
        }
        SCHED();
      }
    }
    STAMP(4);
    mx = x32_max(row_max(mx));
    float mo = mx, m0, m1;
    { float a = mx, c = mx; swap16(a, c); m0 = a; m1 = c; (void)mo; }
    lds_fence();
    f32x4 sa = *(LAS f32x4*)(pw + lane * 8), sb = *(LAS f32x4*)(pw + lane * 8 + 4);
    sa[0] = __builtin_amdgcn_exp2f(sa[0] - m0); sa[1] = __builtin_amdgcn_exp2f(sa[1] - m1);
    sa[2] = __builtin_amdgcn_exp2f(sa[2] - m0); sa[3] = __builtin_amdgcn_exp2f(sa[3] - m1);
    sb[0] = __builtin_amdgcn_exp2f(sb[0] - m0); sb[1] = __builtin_amdgcn_exp2f(sb[1] - m1);
    sb[2] = __builtin_amdgcn_exp2f(sb[2] - m0); sb[3] = __builtin_amdgcn_exp2f(sb[3] - m1);
    *(LAS f32x4*)(pw + lane * 8) = sa; *(LAS f32x4*)(pw + lane * 8 + 4) = sb;
    const float l0 = wave_sum((sa[0] + sa[2]) + (sb[0] + sb[2])), l1s = wave_sum((sa[1] + sa[3]) + (sb[1] + sb[3]));
    if (lane == 0) *(LAS f32x4*)(L + L_ML + w * 16) = f32x4{m0, l0, m1, l1s};
    lds_fence();
    float a0[8], a1[8];
#pragma unroll
    for (int e = 0; e < 8; ++e) { a0[e] = 0.f; a1[e] = 0.f; }
    const LAS f32x2* pr = (const LAS f32x2*)(pw + hf * 2);
    f32x2 pn = pr[0];
#pragma unroll 1
    for (int it = 0; it < 4; ++it) {
      const int sbase = (it + 1) * (CAP * 4096);
#pragma unroll
      for (int i = 0; i < CAP; ++i) {
        const f32x2 pc = pn;
        pn = pr[((it * CAP + i + 1) & 127) * 2];
        const u32x4 vv = ring[i];
#pragma unroll
        for (int e = 0; e < 4; ++e) {
          const float lo = bf_lo(vv[e]), hi = bf_hi(vv[e]);
          a0[2 * e] = fmaf(pc[0], lo, a0[2 * e]); a0[2 * e + 1] = fmaf(pc[0], hi, a0[2 * e + 1]);
          a1[2 * e] = fmaf(pc[1], lo, a1[2 * e]); a1[2 * e + 1] = fmaf(pc[1], hi, a1[2 * e + 1]);
        }
        ring[i] = __builtin_amdgcn_raw_buffer_load_b128(rv, voff, sbase + i * 4096, AUX_NT);
        SCHED();
      }
    }
    STAMP(5);
    f32x4 r0, r1;
#pragma unroll
    for (int e = 0; e < 8; ++e) {
      swap32(a0[e], a1[e]);
      const float t = a0[e] + a1[e];
      if (e < 4) r0[e] = t; else r1[e - 4] = t;
    }
    *(LAS f32x4*)(L + L_MRG + w * 2048 + lane * 32) = r0;
    *(LAS f32x4*)(L + L_MRG + w * 2048 + lane * 32 + 16) = r1;
    wg_barrier();
    return;
  }

  const int hw = w - 4;
  unsigned tag;
  if (hw == 0) {
    const rsrc_t rq = mk_rsrc(p.wq, DM * HD * 2);
    unsigned wr[7][8][1];
    const int qoff = (16 * g4 + 8 * sub) * (HD * 2) + (kvh * 512 + 16 * i32 + 2 * (c16 & 7)) * 2;
#pragma unroll
    for (int kb = 0; kb < 7; ++kb)
#pragma unroll
      for (int jj = 0; jj < 8; ++jj) wr[kb][jj][0] = __builtin_amdgcn_raw_buffer_load_b32(rq, qoff, (64 * kb + jj) * (HD * 2), WAUX);
    SCHED();
    layernorm();
    u32x4 fe[7], fo[7];
#pragma unroll
    for (int kb = 0; kb < 7; ++kb) { fe[kb] = frag_col<1>(wr[kb], 0); fo[kb] = frag_col<1>(wr[kb], 1); }
    wg_barrier();
    STAMP(1);
    tag = *(LAS unsigned*)(L + L_TAG);
    f32x4 de = {0.f, 0.f, 0.f, 0.f}, dd = {0.f, 0.f, 0.f, 0.f};
    const int aoff = L_XN + (c16 & 7) * XN_STRIDE + (16 * g4 + 8 * sub) * 2;
#pragma unroll
    for (int kb = 0; kb < 7; ++kb) {
      const u32x4 af = *(LAS u32x4*)(L + aoff + kb * 128);
      de = mfma16(af, fe[kb], de); dd = mfma16(af, fo[kb], dd);
    }
    unsigned pk[4];
#pragma unroll
    for (int r = 0; r < 4; ++r) {
      const bool valid = (sub == (g4 >> 1));
      float e = valid ? de[r] : 0.f, o = valid ? dd[r] : 0.f;
      e += dpp<XOR8>(e); o += dpp<XOR8>(o);
      e = x32_sum(e); o = x32_sum(o);
      pk[r] = pack_bf16(e, o);
    }
#pragma unroll
    for (int r = 0; r < 4; ++r) {
      const unsigned nb = asu(dpp<XOR1>(asf(pk[r])));
      const int bb = 4 * (g4 & 1) + r;
      const int off = WS_QG + ((bb * HKV + kvh) * 128 + 4 * i32 + ((c16 & 7) >> 1)) * 16;
      if (lane < 32 && (lane & 9) == 0) st_gran(ws, off, pk[r], nb, tag);
    }
    STAMP(2);
  } else {
    unsigned ep = 0;
    if (hw == 3) ep = __builtin_amdgcn_raw_buffer_load_b32(ws, WS_EPOCH, 0, AUX_SC1);
    layernorm();
    if (hw == 3 && lane == 0) *(LAS unsigned*)(L + L_TAG) = ep + 1u;
    wg_barrier();
    STAMP(1);
    tag = *(LAS unsigned*)(L + L_TAG);
    if (hw <= 2) {
      const int gi = (hw - 1) * 64 + lane;
      const int off[1] = {WS_QG + ((b * HKV + kvh) * 128 + gi) * 16};
      u32x4 g[1];
      sweep<1>(ws, off, tag, g);
      *(LAS u32x2*)(L + L_Q + gi * 8) = u32x2{g[0][0], g[0][2]};
    }
    STAMP(2);
  }
  wg_barrier();
  STAMP(3);

  if (hw == 0) {
    const int rb = (i32 >> 3) * 8 + kvh, cb = i32 & 7;
    if (rb < 28) {
      const rsrc_t r1 = mk_rsrc(p.w1, DM * DFF * 2);
      const rsrc_t r2 = mk_rsrc(p.w2, DFF * DM * 2);
      const int o1 = (16 * g4 + 8 * sub) * (DFF * 2) + (64 * rb + 8 * (c16 & 7)) * 2;
      const int aoff = L_XN + (c16 & 7) * XN_STRIDE + (16 * g4 + 8 * sub) * 2;
      f32x4 acc[8];
#pragma unroll
      for (int m = 0; m < 8; ++m) acc[m] = f32x4{0.f, 0.f, 0.f, 0.f};
#pragma unroll 1
      for (int kb = 0; kb < 7; ++kb) {
        unsigned wr[8][4];
#pragma unroll
        for (int jj = 0; jj < 8; ++jj) {
          const u32x4 t = __builtin_amdgcn_raw_buffer_load_b128(r1, o1, (64 * kb + jj) * (DFF * 2), WAUX);
#pragma unroll
          for (int e = 0; e < 4; ++e) wr[jj][e] = t[e];
        }
        const u32x4 af = *(LAS u32x4*)(L + aoff + kb * 128);
#pragma unroll
        for (int m = 0; m < 8; ++m) acc[m] = mfma16(af, frag_col<4>(wr, m), acc[m]);
      }
      unsigned w2r[8][4];
      const int ch = (c16 & 7) < 7 ? (c16 & 7) : 6;
      const int o2 = (64 * rb + 16 * g4 + 8 * sub) * (DM * 2) + (56 * cb + 8 * ch) * 2;
#pragma unroll
      for (int jj = 0; jj < 8; ++jj) {
        const u32x4 t = __builtin_amdgcn_raw_buffer_load_b128(r2, o2, jj * (DM * 2), WAUX);
#pragma unroll
        for (int e = 0; e < 4; ++e) w2r[jj][e] = t[e];
      }
      LAS float* fb = (LAS float*)(L + L_F);
      const bool valid = (sub == (g4 >> 1));
      auto spill_tile = [&]() {
        if (valid) {
#pragma unroll
          for (int r = 0; r < 4; ++r) {
            LAS float* d = fb + (sub * 8 + 4 * (g4 & 1) + r) * 64 + 8 * (c16 & 7);
            *(LAS f32x4*)d = f32x4{acc[0][r], acc[1][r], acc[2][r], acc[3][r]};
            *(LAS f32x4*)(d + 4) = f32x4{acc[4][r], acc[5][r], acc[6][r], acc[7][r]};
          }
        }
        lds_fence();
      };
      spill_tile();
      u32x4 hf;
      {
        const LAS float* s0 = fb + (c16 & 7) * 64 + 16 * g4 + 8 * sub;
        const f32x4 u0 = *(const LAS f32x4*)s0 + *(const LAS f32x4*)(s0 + 512);
        const f32x4 u1 = *(const LAS f32x4*)(s0 + 4) + *(const LAS f32x4*)(s0 + 516);
        hf[0] = pack_bf16(fmaxf(u0[0], 0.f), fmaxf(u0[1], 0.f)); hf[1] = pack_bf16(fmaxf(u0[2], 0.f), fmaxf(u0[3], 0.f));
        hf[2] = pack_bf16(fmaxf(u1[0], 0.f), fmaxf(u1[1], 0.f)); hf[3] = pack_bf16(fmaxf(u1[2], 0.f), fmaxf(u1[3], 0.f));
      }
      lds_fence();
#pragma unroll
      for (int m = 0; m < 8; ++m) acc[m] = mfma16(hf, frag_col<4>(w2r, m), f32x4{0.f, 0.f, 0.f, 0.f});
      spill_tile();
#pragma unroll
      for (int i = 0; i < 4; ++i) {
        const int idx = lane + 64 * i;
        if (idx < 224) {
          const int bb = idx / 28, pp = idx % 28;
          const f32x2 v = *(const LAS f32x2*)(fb + bb * 64 + 2 * pp) + *(const LAS f32x2*)(fb + 512 + bb * 64 + 2 * pp);
          st_gran(ws, WS_GG + ((rb * NB + bb) * 224 + cb * 28 + pp) * 16, asu(v[0]), asu(v[1]), tag);
        }
      }
    }
    STAMP(4);
  } else if (hw == 2 && blk == 0) {
    int off[4];
    u32x4 g[4];
#pragma unroll
    for (int i = 0; i < 4; ++i) { const int s = lane + 64 * i; off[i] = WS_QG + ((s & 7) * 128 + 4 * (s >> 3)) * 16; }
    sweep<4>(ws, off, tag, g);
    if (lane == 0) __builtin_amdgcn_raw_buffer_store_b32(tag, ws, WS_EPOCH, 0, AUX_SC1);
  }

  unsigned wp[16][8];
  {
    const rsrc_t ro = mk_rsrc(p.wo, HD * DM * 2);
    const int oo = (kvh * 512 + 128 * j + 32 * hw) * (DM * 2) + (lane < 56 ? lane : 55) * 16;
#pragma unroll
    for (int r = 0; r < 16; ++r) {
      const u32x4 ra = __builtin_amdgcn_raw_buffer_load_b128(ro, oo, (2 * r) * (DM * 2), WAUX);
      const u32x4 rb2 = __builtin_amdgcn_raw_buffer_load_b128(ro, oo, (2 * r + 1) * (DM * 2), WAUX);
#pragma unroll
      for (int e = 0; e < 4; ++e) { wp[r][2 * e] = perm_lo(ra[e], rb2[e]); wp[r][2 * e + 1] = perm_hi(ra[e], rb2[e]); }
    }
  }
  const int fi = kvh * 4 + j;
  const int fgi = lane >> 3, fsg = lane & 7;
  float gate0 = 0.f, gate1 = 0.f, x0 = 0.f, x1 = 0.f;
  if (hw == 1) {
    int off[4];
    u32x4 g[4];
    const int gi = fgi < 7 ? fgi : 6, sg = fsg < 7 ? fsg : 6;
#pragma unroll
    for (int i = 0; i < 4; ++i) off[i] = WS_GG + (((sg * 4 + i) * NB + b) * 224 + fi * 7 + gi) * 16;
    const unsigned xx = __builtin_amdgcn_raw_buffer_load_b32(rx, b * (DM * 2) + (fi * 7 + gi) * 4, 0, 0);
    sweep<4>(ws, off, tag, g);
    float s0 = 0.f, s1 = 0.f;
#pragma unroll
    for (int i = 0; i < 4; ++i) { s0 += asf(g[i][0]); s1 += asf(g[i][2]); }
    if (fsg == 7) { s0 = 0.f; s1 = 0.f; }
    s0 += dpp<XOR1>(s0); s0 += dpp<XOR2>(s0); s0 += dpp<XOR7>(s0);
    s1 += dpp<XOR1>(s1); s1 += dpp<XOR2>(s1); s1 += dpp<XOR7>(s1);
    s0 = rnd_bf16(s0); s1 = rnd_bf16(s1);
    gate0 = rnd_bf16(s0 / (1.0f + __expf(-s0))); gate1 = rnd_bf16(s1 / (1.0f + __expf(-s1)));
    x0 = bf_lo(xx); x1 = bf_hi(xx);
  }
#pragma unroll
  for (int r = 0; r < 16; ++r)
#pragma unroll
    for (int e = 0; e < 8; ++e) asm volatile("" : "+v"(wp[r][e]));
  STAMP(5);
  wg_barrier();
  STAMP(6);
  {
    const int t = hw * 64 + lane, head = hw >> 1;
    float mw[4], lw[4];
#pragma unroll
    for (int ww = 0; ww < 4; ++ww) { const f32x2 ml = *(const LAS f32x2*)(L + L_ML + ww * 16 + head * 8); mw[ww] = ml[0]; lw[ww] = ml[1]; }
    const float M = fmaxf(fmaxf(mw[0], mw[1]), fmaxf(mw[2], mw[3]));
    float Ls = 0.f, v0 = 0.f, v1 = 0.f;
#pragma unroll
    for (int ww = 0; ww < 4; ++ww) {
      const float f = __builtin_amdgcn_exp2f(mw[ww] - M);
      const f32x2 av = *(const LAS f32x2*)(L + L_MRG + ww * 2048 + t * 8);
      Ls = fmaf(f, lw[ww], Ls); v0 = fmaf(f, av[0], v0); v1 = fmaf(f, av[1], v1);
    }
    st_gran(ws, WS_S1 + (((b * HKV + kvh) * 4 + j) * 256 + t) * 16, asu(v0), asu(v1), tag);
    if (lane == 0 && (hw & 1) == 0)
      st_gran(ws, WS_ML + (((b * HKV + kvh) * 2 + head) * 4 + j) * 16, asu(M), asu(Ls), tag);
  }
  STAMP(7);
  {
    const int off[2] = {WS_S1 + (((b * HKV + kvh) * 4 + g4) * 256 + 64 * j + 16 * hw + c16) * 16,
                        WS_ML + (((b * HKV + kvh) * 2 + (j >> 1)) * 4 + g4) * 16};
    u32x4 g[2];
    sweep<2>(ws, off, tag, g);
    STAMP(8);
    const float ms = asf(g[1][0]), ls = asf(g[1][2]);
    const float M = x32_max(x16_max(ms));
    const float f = __builtin_amdgcn_exp2f(ms - M);
    const float Lt = x32_sum(x16_sum(f * ls));
    const float v0 = x32_sum(x16_sum(f * asf(g[0][0])));
    const float v1 = x32_sum(x16_sum(f * asf(g[0][2])));
    const float inv = __builtin_amdgcn_rcpf(Lt);
    const unsigned opk = pack_bf16(v0 * inv, v1 * inv);
    float sacc[8];
#pragma unroll
    for (int e = 0; e < 8; ++e) sacc[e] = 0.f;
#pragma unroll
    for (int r = 0; r < 16; ++r) {
      const unsigned ov = __builtin_amdgcn_readlane(opk, r);
#pragma unroll
      for (int e = 0; e < 8; ++e) sacc[e] = dot2(wp[r][e], ov, sacc[e]);
    }
    if (lane < 56) {
      *(LAS f32x4*)(L + L_S + hw * 1792 + lane * 32) = f32x4{sacc[0], sacc[1], sacc[2], sacc[3]};
      *(LAS f32x4*)(L + L_S + hw * 1792 + lane * 32 + 16) = f32x4{sacc[4], sacc[5], sacc[6], sacc[7]};
    }
  }
  wg_barrier();
  STAMP(9);
  {
    const int t = hw * 64 + lane;
    if (t < 224) {
      f32x2 v = *(const LAS f32x2*)(L + L_S + t * 8);
#pragma unroll
      for (int ww = 1; ww < 4; ++ww) v += *(const LAS f32x2*)(L + L_S + ww * 1792 + t * 8);
      st_gran(ws, WS_S2 + ((b * 32 + fi) * 224 + t) * 16, asu(v[0]), asu(v[1]), tag);
    }
  }
  if (hw == 1) {
    int off[4];
    u32x4 g[4];
    const int gi = fgi < 7 ? fgi : 6;
#pragma unroll
    for (int i = 0; i < 4; ++i) off[i] = WS_S2 + ((b * 32 + fsg * 4 + i) * 224 + fi * 7 + gi) * 16;
    sweep<4>(ws, off, tag, g);
    STAMP(10);
    float s0 = 0.f, s1 = 0.f;
#pragma unroll
    for (int i = 0; i < 4; ++i) { s0 += asf(g[i][0]); s1 += asf(g[i][2]); }
    s0 += dpp<XOR1>(s0); s0 += dpp<XOR2>(s0); s0 += dpp<XOR7>(s0);
    s1 += dpp<XOR1>(s1); s1 += dpp<XOR2>(s1); s1 += dpp<XOR7>(s1);
    const float o0 = x0 + rnd_bf16(rnd_bf16(s0) * gate0), o1 = x1 + rnd_bf16(rnd_bf16(s1) * gate1);
    const rsrc_t rout = mk_rsrc(p.out, NB * DM * 2);
    if (fsg == 0 && fgi < 7) __builtin_amdgcn_raw_buffer_store_b32(pack_bf16(o0, o1), rout, b * (DM * 2) + (fi * 7 + fgi) * 4, 0, 0);
    STAMP(11);
  }
}

}

extern "C" void kernel_launch(void* const* d_in, const int* in_sizes, int n_in, void* d_out, int out_size, void* d_ws,
                              size_t ws_size, hipStream_t stream) {
  (void)in_sizes; (void)n_in; (void)out_size; (void)ws_size;
  Params p;
  p.x = d_in[0]; p.k = d_in[1]; p.v = d_in[2]; p.wq = d_in[3]; p.wo = d_in[4]; p.w1 = d_in[6]; p.w2 = d_in[7];
  p.out = d_out; p.ws = d_ws;
  (void)hipFuncSetAttribute((const void*)fused_decode_kernel, hipFuncAttributeMaxDynamicSharedMemorySize, LDS_BYTES);
  hipLaunchKernelGGL(fused_decode_kernel, dim3(NWG), dim3(NTHR), LDS_BYTES, stream, p);
}
```

```cpp
#include <hip/hip_runtime.h>
#include <hip/hip_bf16.h>
#include <cstdint>

namespace {

typedef unsigned u32x4 __attribute__((ext_vector_type(4)));
typedef unsigned u32x2 __attribute__((ext_vector_type(2)));
typedef float f32x4 __attribute__((ext_vector_type(4)));
typedef float f32x2 __attribute__((ext_vector_type(2)));
typedef short bf16x8 __attribute__((ext_vector_type(8)));
typedef __bf16 bf16x2 __attribute__((ext_vector_type(2)));
#define LAS __attribute__((address_space(3)))
typedef __amdgpu_buffer_rsrc_t rsrc_t;

constexpr int NB = 8, DM = 448, HKV = 8, DFF = 1792, HD = 4096;
constexpr int NWG = 256, NTHR = 512;
constexpr int AUX_NT = 2, AUX_SC1 = 16, RSRC_W3 = 0x00020000;
#define TTOT 3730
constexpr int TSTART = 50;
constexpr int LEAD = 4;
constexpr int TEXTRA = 160;
constexpr int TTOT0 = TTOT + TEXTRA * 256 / (64 - LEAD);
constexpr int PH = 60;
constexpr int PACE0 = 8, PACE = 14;
constexpr int Q_T = 720;
constexpr int WO_T0 = 900, WO_DT = 50;
constexpr int ML_T = 3000;
constexpr int S1T = 60, S2T = 70;
constexpr int K0 = 35, PNOM = 410, BETA = 10, GAMMA = 8;
constexpr float C2 = 0.0625f * 1.4426950408889634f;
constexpr int CHUNK = 512 * 1024;
constexpr unsigned SPIN_CAP = 1u << 18;

constexpr int WS_EPOCH = 0;
constexpr int WS_QG = 4096;
constexpr int WS_HG = WS_QG + NB * HKV * 128 * 16;
constexpr int WS_GG = WS_HG + 7 * NB * 64 * 16;
constexpr int WS_ML = WS_GG + NB * 7 * 112 * 16;
constexpr int WS_SB = WS_ML + NB * HKV * 32 * 16;
constexpr int WS_S1 = WS_SB + NB * HKV * 4 * 64 * 16;
constexpr int WS_S2 = WS_S1 + NB * HKV * 4 * 128 * 16;
constexpr int WS_END = WS_S2 + NB * 32 * 112 * 16;

constexpr int XN_STRIDE = 912;
constexpr int L_K = 0;
constexpr int L_MRG = L_K;
constexpr int L_AB = L_K + 8192;
constexpr int L_XN = L_K + 4 * 32768;
constexpr int L_FLAG = L_XN + 8 * XN_STRIDE;
constexpr int L_FLAGT = L_FLAG, L_FLAGA = L_FLAG + 4, L_FLAGB = L_FLAG + 8, L_FLAGQ = L_FLAG + 12, L_FLAGK = L_FLAG + 16,
              L_FLAGP = L_FLAG + 20, L_FLAGV = L_FLAG + 24, L_SHIFT = L_FLAG + 32;
constexpr int L_TAG = L_FLAG + 48;
constexpr int L_GATE = L_TAG + 16;
constexpr int L_Q = L_GATE + 512;
constexpr int L_P = L_Q + 1024;
constexpr int L_ML = L_P + 4 * 2048;
constexpr int L_RED = L_ML + 64;
constexpr int L_PB = L_RED + 8192;
constexpr int L_END = L_PB + 4096;
constexpr int LDS_BYTES = L_END;
static_assert(LDS_BYTES <= 160 * 1024 && LDS_BYTES > 80 * 1024, "lds");

struct Params {
  const void* x; const void* k; const void* v; const void* wq; const void* wo; const void* w1; const void* w2;
  void* out; void* ws;
};

__device__ __forceinline__ rsrc_t mk_rsrc(const void* p, int bytes) {
  return __builtin_amdgcn_make_buffer_rsrc(const_cast<void*>(p), 0, bytes, RSRC_W3);
}
__device__ __forceinline__ float asf(unsigned u) { return __builtin_bit_cast(float, u); }
__device__ __forceinline__ unsigned asu(float f) { return __builtin_bit_cast(unsigned, f); }
__device__ __forceinline__ float bf_lo(unsigned u) { return asf(u << 16); }
__device__ __forceinline__ float bf_hi(unsigned u) { return asf(u & 0xffff0000u); }
__device__ __forceinline__ unsigned pack_bf16(float lo, float hi) {
  bf16x2 r; r[0] = (__bf16)lo; r[1] = (__bf16)hi;
  return __builtin_bit_cast(unsigned, r);
}
__device__ __forceinline__ float rnd_bf16(float f) { return (float)(__bf16)f; }
__device__ __forceinline__ float dot2(unsigned a, unsigned b, float c) {
  return __builtin_amdgcn_fdot2_f32_bf16(__builtin_bit_cast(bf16x2, a), __builtin_bit_cast(bf16x2, b), c, false);
}
__device__ __forceinline__ unsigned perm_lo(unsigned a, unsigned b) { return __builtin_amdgcn_perm(b, a, 0x05040100u); }
__device__ __forceinline__ unsigned perm_hi(unsigned a, unsigned b) { return __builtin_amdgcn_perm(b, a, 0x07060302u); }
template <int CTRL> __device__ __forceinline__ float dpp(float x) {
  return asf((unsigned)__builtin_amdgcn_mov_dpp((int)asu(x), CTRL, 0xf, 0xf, true));
}
constexpr int XOR1 = 0xB1, XOR2 = 0x4E, XOR7 = 0x141, XOR8 = 0x128, ROR4 = 0x124;
__device__ __forceinline__ void swap16(float& a, float& b) {
  auto s = __builtin_amdgcn_permlane16_swap(asu(a), asu(b), false, false);
  a = asf(s[0]); b = asf(s[1]);
}
__device__ __forceinline__ void swap32(float& a, float& b) {
  auto s = __builtin_amdgcn_permlane32_swap(asu(a), asu(b), false, false);
  a = asf(s[0]); b = asf(s[1]);
}
__device__ __forceinline__ float x16_sum(float x) { float a = x, b = x; swap16(a, b); return a + b; }
__device__ __forceinline__ float x32_sum(float x) { float a = x, b = x; swap32(a, b); return a + b; }
__device__ __forceinline__ float x16_max(float x) { float a = x, b = x; swap16(a, b); return fmaxf(a, b); }
__device__ __forceinline__ float x32_max(float x) { float a = x, b = x; swap32(a, b); return fmaxf(a, b); }
__device__ __forceinline__ float row_sum(float x) { x += dpp<XOR1>(x); x += dpp<XOR2>(x); x += dpp<XOR7>(x); x += dpp<XOR8>(x); return x; }
__device__ __forceinline__ float row_max(float x) {
  x = fmaxf(x, dpp<XOR1>(x)); x = fmaxf(x, dpp<XOR2>(x)); x = fmaxf(x, dpp<XOR7>(x)); x = fmaxf(x, dpp<XOR8>(x)); return x;
}
__device__ __forceinline__ float wave_sum(float x) { return x32_sum(x16_sum(row_sum(x))); }
__device__ __forceinline__ f32x4 mfma16(u32x4 a, u32x4 b, f32x4 c) {
  return __builtin_amdgcn_mfma_f32_16x16x32_bf16(__builtin_bit_cast(bf16x8, a), __builtin_bit_cast(bf16x8, b), c, 0, 0, 0);
}
__device__ __forceinline__ void wg_barrier() { asm volatile("s_waitcnt lgkmcnt(0)\n\ts_barrier" ::: "memory"); }
__device__ __forceinline__ void lds_fence() { asm volatile("s_waitcnt lgkmcnt(0)" ::: "memory"); }
#define SCHED() __builtin_amdgcn_sched_barrier(0)

constexpr int OOB = 0x7fffff00;
__device__ __forceinline__ unsigned tagf(unsigned tag, int i) { return (tag + (i ? tag >> (10 * i) : 0u) + 0x1A5u * i) & 0xFFFu; }
__device__ __forceinline__ void st_gran4(rsrc_t ws, int off, f32x4 v, unsigned tag, unsigned side = 0u) {
  u32x4 g;
#pragma unroll
  for (int i = 0; i < 4; ++i) g[i] = ((asu(v[i]) + 0x800u) & 0xFFFFF000u) | tagf(tag, i) | (((side >> (2 * i)) & 3u) << 12);
  __builtin_amdgcn_raw_buffer_store_b128(g, ws, off, 0, AUX_SC1);
}
__device__ __forceinline__ void st_gran2(rsrc_t ws, int off, float v0, float v1, unsigned tag) {
  __builtin_amdgcn_raw_buffer_store_b128(u32x4{asu(v0), tag, asu(v1), ~tag}, ws, off, 0, AUX_SC1);
}
__device__ __forceinline__ bool ok4(u32x4 g, unsigned tag) {
  return (((g[0] ^ tagf(tag, 0)) | (g[1] ^ tagf(tag, 1)) | (g[2] ^ tagf(tag, 2)) | (g[3] ^ tagf(tag, 3))) & 0xFFFu) == 0u;
}
__device__ __forceinline__ unsigned side8(u32x4 g) {
  return ((g[0] >> 12) & 3u) | ((g[1] >> 10) & 12u) | ((g[2] >> 8) & 48u) | ((g[3] >> 6) & 192u);
}
__device__ __forceinline__ bool ok2(u32x4 g, unsigned tag) { return g[1] == tag && g[3] == ~tag; }
__device__ __forceinline__ f32x4 val4(u32x4 g) {
  return f32x4{asf(g[0] & 0xFFFFF000u), asf(g[1] & 0xFFFFF000u), asf(g[2] & 0xFFFFF000u), asf(g[3] & 0xFFFFF000u)};
}
template <int N, int N4, int SLP, int SKIP = 0> __device__ __forceinline__ void sweep(rsrc_t ws, const int (&off)[N], unsigned tag, u32x4 (&g)[N]) {
  int o[N];
#pragma unroll
  for (int i = 0; i < N; ++i) { o[i] = off[i]; g[i] = u32x4{0u, 0u, 0u, 0u}; }
#pragma unroll 1
  for (unsigned spins = 0; spins < SPIN_CAP; ++spins) {
    asm volatile("" ::: "memory");
    u32x4 t[N];
#pragma unroll
    for (int i = 0; i < N; ++i) t[i] = __builtin_amdgcn_raw_buffer_load_b128(ws, o[i], 0, AUX_SC1);
    bool ok = true;
#pragma unroll
    for (int i = 0; i < N; ++i) {
      const bool hit = (i < N4 ? ok4(t[i], tag) : ok2(t[i], tag)) && o[i] != OOB;
#pragma unroll
      for (int e = 0; e < 4; ++e) g[i][e] = hit ? t[i][e] : g[i][e];
      o[i] = hit ? OOB : o[i];
      ok &= (o[i] == OOB);
    }
    if (__all(ok)) break;
    __builtin_amdgcn_s_sleep(SLP);
  }
}
#define PP_LD(R0, R1) "buffer_load_dwordx4 v[" R0 "], %[o0], %[rs], 0 offen sc1\n\tbuffer_load_dwordx4 v[" R1 "], %[o1], %[rs], 0 offen sc1\n\t"
#define PP_CK(A, B, C, D, E, F, G, H, LBL) \
  "s_waitcnt vmcnt(4)\n\t" \
  "v_xor_b32 %[r0], %[f0], v" A "\n\tv_xor_b32 %[r1], %[f0], v" E "\n\t" \
  "v_bitop3_b32 %[r0], v" B ", %[f1], %[r0] bitop3:0xbe\n\tv_bitop3_b32 %[r1], v" F ", %[f1], %[r1] bitop3:0xbe\n\t" \
  "v_bitop3_b32 %[r0], v" C ", %[f2], %[r0] bitop3:0xbe\n\tv_bitop3_b32 %[r1], v" G ", %[f2], %[r1] bitop3:0xbe\n\t" \
  "v_bitop3_b32 %[r0], v" D ", %[f3], %[r0] bitop3:0xbe\n\tv_bitop3_b32 %[r1], v" H ", %[f3], %[r1] bitop3:0xbe\n\t" \
  "v_or_b32 %[r0], %[r0], %[r1]\n\tv_and_b32 %[r0], 0xfff, %[r0]\n\t" \
  "v_cmp_ne_u32 vcc, 0, %[r0]\n\t" \
  "s_cbranch_vccz " LBL "%=\n\t"
#define PP_OUT(A, B, C, D, E, F, G, H) \
  "v_and_b32 %[r0], %[mk], v" A "\n\tv_and_b32 %[r1], %[mk], v" B "\n\tv_and_b32 %[r2], %[mk], v" C "\n\tv_and_b32 %[r3], %[mk], v" D "\n\t" \
  "v_and_b32 v" E ", %[mk], v" E "\n\tv_and_b32 v" F ", %[mk], v" F "\n\tv_and_b32 v" G ", %[mk], v" G "\n\tv_and_b32 v" H ", %[mk], v" H "\n\t" \
  "v_add_f32 %[r0], %[r0], v" E "\n\tv_add_f32 %[r1], %[r1], v" F "\n\tv_add_f32 %[r2], %[r2], v" G "\n\tv_add_f32 %[r3], %[r3], v" H "\n\t"
#define PP_REGS "v200", "v201", "v202", "v203", "v204", "v205", "v206", "v207", "v208", "v209", "v210", "v211", \
                "v212", "v213", "v214", "v215", "v216", "v217", "v218", "v219", "v220", "v221", "v222", "v223"
__device__ __forceinline__ f32x4 poll_pipe(rsrc_t ws, int o0, int o1, unsigned tag) {
  float r0, r1, r2, r3;
  unsigned cnt = SPIN_CAP;
  asm volatile(
      "s_nop 4\n\t"
      PP_LD("200:203", "204:207") "s_sleep %[sl]\n\t"
      PP_LD("208:211", "212:215") "s_sleep %[sl]\n\t"
      PP_LD("216:219", "220:223")
      ".Lpp_loop%=:\n\t"
      PP_CK("200", "201", "202", "203", "204", "205", "206", "207", ".Lpp_a")
      PP_LD("200:203", "204:207")
      PP_CK("208", "209", "210", "211", "212", "213", "214", "215", ".Lpp_b")
      PP_LD("208:211", "212:215")
      PP_CK("216", "217", "218", "219", "220", "221", "222", "223", ".Lpp_c")
      PP_LD("216:219", "220:223")
      "s_sub_u32 %[cnt], %[cnt], 1\n\t"
      "s_cbranch_scc0 .Lpp_loop%=\n\t"
      "s_waitcnt vmcnt(0)\n"
      ".Lpp_a%=:\n\t"
      PP_OUT("200", "201", "202", "203", "204", "205", "206", "207")
      "s_branch .Lpp_end%=\n"
      ".Lpp_b%=:\n\t"
      PP_OUT("208", "209", "210", "211", "212", "213", "214", "215")
      "s_branch .Lpp_end%=\n"
      ".Lpp_c%=:\n\t"
      PP_OUT("216", "217", "218", "219", "220", "221", "222", "223")
      ".Lpp_end%=:\n"
      ".Lfdk_tail:\n\t"
      "s_nop 1"
      : [r0] "=&v"(r0), [r1] "=&v"(r1), [r2] "=&v"(r2), [r3] "=&v"(r3), [cnt] "+s"(cnt)
      : [o0] "v"(o0), [o1] "v"(o1), [rs] "s"(ws), [f0] "s"(tagf(tag, 0)), [f1] "s"(tagf(tag, 1)), [f2] "s"(tagf(tag, 2)),
        [f3] "s"(tagf(tag, 3)), [mk] "s"(0xFFFFF000u), [sl] "n"(3)
      : PP_REGS, "vcc", "scc", "memory");
  return f32x4{r0, r1, r2, r3};
}

__device__ __forceinline__ void put_rows(LAS unsigned char* row, int Q, f32x4 v) {
  LAS unsigned short* d = (LAS unsigned short*)row + (Q >> 3) * 32 + (Q & 7);
  const unsigned lo = pack_bf16(v[0], v[1]), hi = pack_bf16(v[2], v[3]);
  d[0] = (unsigned short)lo; d[8] = (unsigned short)(lo >> 16); d[16] = (unsigned short)hi; d[24] = (unsigned short)(hi >> 16);
}
__device__ __forceinline__ u32x4 frag_col(const u32x4 (&r)[8], int m) {
  u32x4 f;
#pragma unroll
  for (int i = 0; i < 4; ++i)
    f[i] = (m & 1) ? perm_hi(r[2 * i][m >> 1], r[2 * i + 1][m >> 1]) : perm_lo(r[2 * i][m >> 1], r[2 * i + 1][m >> 1]);
  return f;
}
template <int SLP> __device__ __forceinline__ void lds_wait_ge(LAS unsigned char* L, int off, unsigned want) {
#pragma unroll 1
  for (unsigned spins = 0; spins < (SPIN_CAP << 4); ++spins) {
    const unsigned v = __hip_atomic_load((LAS unsigned*)(L + off), __ATOMIC_RELAXED, __HIP_MEMORY_SCOPE_WORKGROUP);
    if (__builtin_amdgcn_readfirstlane(v) >= want) break;
    __builtin_amdgcn_s_sleep(SLP);
  }
  asm volatile("" ::: "memory");
}
__device__ __forceinline__ void lds_signal(LAS unsigned char* L, int off, int lane) {
  lds_fence();
  if (lane == 0) __hip_atomic_fetch_add((LAS unsigned*)(L + off), 1u, __ATOMIC_RELAXED, __HIP_MEMORY_SCOPE_WORKGROUP);
}
__device__ __forceinline__ float red4(f32x4 v) {
  float a = v[0], c = v[1], d = v[2], e = v[3];
  swap32(a, c); swap32(d, e);
  float u = a + c, x = d + e;
  swap16(u, x);
  float t = u + x;
  t += dpp<ROR4>(t); t += dpp<XOR8>(t);
  return t;
}
__device__ __forceinline__ void throttle(unsigned due) {
  asm volatile(
      ".Lthr%=:\n\t"
      "s_memrealtime s[100:101]\n\t"
      "s_waitcnt lgkmcnt(0)\n\t"
      "s_sub_u32 s100, s100, %0\n\t"
      "s_cmp_lt_i32 s100, 0\n\t"
      "s_cbranch_scc1 .Lthr%="
      ::"s"(__builtin_amdgcn_readfirstlane(due)) : "s100", "s101", "scc", "memory");
}
__device__ __forceinline__ void wait_until(unsigned due) {
  asm volatile(
      ".Lwu%=:\n\t"
      "s_sleep 2\n\t"
      "s_memrealtime s[100:101]\n\t"
      "s_waitcnt lgkmcnt(0)\n\t"
      "s_sub_u32 s100, s100, %0\n\t"
      "s_cmp_lt_i32 s100, 0\n\t"
      "s_cbranch_scc1 .Lwu%="
      ::"s"(__builtin_amdgcn_readfirstlane(due)) : "s100", "s101", "scc", "memory");
}
#define DUE(n) (ts + TSTART + TEXTRA + ((unsigned)((n) - LEAD) * TTOT >> 8))
#define DUE0(n) (ts + TSTART + ((unsigned)((n) - LEAD) * TTOT0 >> 8))
#define THROTTLE(i) do { if (((i) & 3) == 0) { throttle(dv0 + (dvo >> 6)); dvo += dstep; } } while (0)
#define PACE_WAIT() asm volatile("s_waitcnt vmcnt(%0)" ::"n"(PACE - 1) : "memory")

__global__ void __launch_bounds__(NTHR, 2) fused_decode_kernel(Params p) {
  extern __shared__ __attribute__((aligned(16))) unsigned char smem[];
  LAS unsigned char* L = (LAS unsigned char*)smem;
  const unsigned tw = (unsigned)__builtin_amdgcn_s_memrealtime();
  const int tid = threadIdx.x, lane = tid & 63, w = __builtin_amdgcn_readfirstlane(tid >> 6);
  const int blk = blockIdx.x, i32 = blk >> 3, b = blk >> 5;
  const int xr = blk & 7, jr = i32 & 3, gr = b * HKV + xr;
  const int kvh = xr, j = jr;
  const int grp = b * HKV + kvh;
  if (tid < 4) *(LAS u32x4*)(L + L_FLAG + tid * 16) = u32x4{0u, tid == 3 ? tw : 0u, 0u, 0u};
  asm volatile("" ::"s"(p.ws), "s"(p.x), "s"(p.wq), "s"(p.k), "s"(p.v), "s"(p.out));
  const rsrc_t ws = mk_rsrc(p.ws, WS_END);
  const int c16 = lane & 15, g4 = lane >> 4, sub = c16 >> 3;

  const unsigned stag = (unsigned)((w & 3) * 8 + (i32 & 7)) * PH >> 5;
  const size_t kv_off = ((size_t)grp * 4 + j) * CHUNK;
  const rsrc_t rk = mk_rsrc((const char*)p.k + kv_off, CHUNK);
  const int voff = w * 1024 + lane * 16;
  {
    unsigned lpos = (unsigned)(uintptr_t)(L + L_K) + w * 32768;
    unsigned gpos = w < 4 ? 0u : 32u * 4096u;
    unsigned due = (tw - stag + TSTART) * 64u - (LEAD / 4 - 1) * TTOT0;
#define DMA1 "s_waitcnt vmcnt(%5)\n\ts_mov_b32 m0, %0\n\ts_add_u32 %0, %0, 0x400\n\t" \
             "buffer_load_dwordx4 %3, %4, %1 offen nt lds\n\ts_add_u32 %1, %1, 0x1000\n\t"
    asm volatile(
        "s_nop 4\n\t"
        "s_cmp_ge_u32 %1, %6\n\t"
        "s_cbranch_scc1 .Ldma_end%=\n\t"
        DMA1 DMA1 DMA1 DMA1
        "s_waitcnt lgkmcnt(0)\n\t"
        "s_barrier\n"
        ".Ldma_loop%=:\n\t"
        "s_memrealtime s[100:101]\n\t"
        "s_waitcnt lgkmcnt(0)\n\t"
        "s_lshl_b32 s100, s100, 6\n\t"
        "s_sub_u32 s100, s100, %2\n\t"
        "s_cmp_lt_i32 s100, 0\n\t"
        "s_cbranch_scc1 .Ldma_loop%=\n\t"
        DMA1 DMA1 DMA1 DMA1
        "s_add_u32 %2, %2, %7\n\t"
        "s_cmp_lt_u32 %1, %6\n\t"
        "s_cbranch_scc1 .Ldma_loop%=\n"
        ".Ldma_end%=:"
        : "+s"(lpos), "+s"(gpos), "+s"(due)
        : "v"(voff), "s"(rk), "n"(PACE0 - 1), "n"(32 * 4096), "s"(TTOT0)
        : "m0", "scc", "s100", "s101", "memory");
  }

  if (w < 4) {
    const unsigned ts = __builtin_amdgcn_readfirstlane(*(LAS unsigned*)(L + L_TAG + 4)) - stag;
    const rsrc_t rv = mk_rsrc((const char*)p.v + kv_off, CHUNK);
    u32x4 ring[32];
#pragma unroll
    for (int i = 0; i < 32; ++i) {
      if ((i & 3) == 0) throttle(DUE0(32 + i));
      PACE_WAIT();
      ring[i] = __builtin_amdgcn_raw_buffer_load_b128(rk, voff, (32 + i) * 4096, AUX_NT);
      SCHED();
    }
    lds_wait_ge<1>(L, L_FLAGQ, 2u);
    const int hf = lane >> 5;
    const u32x4 q0 = *(LAS u32x4*)(L + L_Q + (lane & 31) * 16), q1 = *(LAS u32x4*)(L + L_Q + 512 + (lane & 31) * 16);
    LAS float* pw = (LAS float*)(L + L_P + w * 2048);
    const int islot = ((lane >> 3) & 1) | ((lane >> 1) & 2) | ((lane << 1) & 4) | ((lane << 3) & 8);
    const int pidx = (islot >> 1) * 8 + hf * 4 + (islot & 1) * 2 + (g4 & 1);
    const int pidx0 = (islot >> 2) * 8 + hf * 4 + ((islot >> 1) & 1) * 2 + (g4 & 1);
    const unsigned dv0 = DUE(64);
    const int sh0 = __builtin_amdgcn_readfirstlane(*(LAS int*)(L + L_SHIFT)), sh1 = __builtin_amdgcn_readfirstlane(*(LAS int*)(L + L_SHIFT + 4));
    const int sh2 = (__builtin_amdgcn_readfirstlane(*(LAS int*)(L + L_SHIFT + 8)) - 128) * 128;
    const unsigned dstep = TTOT + (sh0 * 16 + max(sh1 - sh0, 0) * BETA + (sh2 - sh0) * GAMMA) / (47 * 8);
    unsigned dvo = 0;
    float mx = -INFINITY;
    float l1[2], l2[2], l3[2], l4[2];
    auto qk = [&](const u32x4 kk, float& a, float& c) {
      a = dot2(kk[0], q0[0], 0.f); c = dot2(kk[0], q1[0], 0.f);
#pragma unroll
      for (int e = 1; e < 4; ++e) { a = dot2(kk[e], q0[e], a); c = dot2(kk[e], q1[e], c); }
    };
    auto tree = [&](float a, float c, const int i, LAS float* dst) {
      swap16(a, c);
      l1[i & 1] = a + c;
      if ((i & 1) == 1) {
        const float t1 = l1[0] + dpp<XOR8>(l1[0]), t2 = l1[1] + dpp<XOR8>(l1[1]);
        l2[(i >> 1) & 1] = (lane & 8) ? t2 : t1;
      }
      if ((i & 3) == 3) {
        const float t1 = l2[0] + dpp<XOR7>(l2[0]), t2 = l2[1] + dpp<XOR7>(l2[1]);
        l3[(i >> 2) & 1] = (lane & 4) ? t2 : t1;
      }
      if ((i & 7) == 7) {
        const float t1 = l3[0] + dpp<XOR2>(l3[0]), t2 = l3[1] + dpp<XOR2>(l3[1]);
        l4[(i >> 3) & 1] = (lane & 2) ? t2 : t1;
      }
      if ((i & 15) == 15) {
        const float t1 = l4[0] + dpp<XOR1>(l4[0]), t2 = l4[1] + dpp<XOR1>(l4[1]);
        const float sc = rnd_bf16((lane & 1) ? t2 : t1);
        mx = fmaxf(mx, sc);
        *dst = sc;
      }
    };
    {
      {
        LAS float* dst = pw + pidx0 + ((islot & 1) ? 32 : 48) * 4;
#pragma unroll
        for (int i = 0; i < 16; ++i) {
          float a0, c0, a1, c1;
          qk(ring[i], a1, c1);
          asm volatile("" : "+v"(a1), "+v"(c1));
          THROTTLE(i);
          PACE_WAIT();
          ring[i] = __builtin_amdgcn_raw_buffer_load_b128(rk, voff, (64 + i) * 4096, AUX_NT);
          qk(ring[16 + i], a0, c0);
          tree(a0, c0, (2 * i) & 15, pw);
          tree(a1, c1, (2 * i + 1) & 15, dst + (i >> 3) * 32);
          SCHED();
        }
      }
      const LAS u32x4* kl = (const LAS u32x4*)(L + L_K + w * 32768 + lane * 16);
      u32x4 kn = kl[0];
      LAS float* dst = pw + pidx0 + ((islot & 1) ? 64 : 0) * 4;
#pragma unroll 1
      for (int it = 0; it < 2; ++it) {
#pragma unroll
        for (int i = 0; i < 16; ++i) {
          float a0, c0, a1, c1;
          const u32x4 kc = kn;
          kn = kl[((it * 16 + i + 1) & 31) * 64];
          qk(ring[i], a1, c1);
          asm volatile("" : "+v"(a1), "+v"(c1));
          THROTTLE(i);
          PACE_WAIT();
          ring[i] = __builtin_amdgcn_raw_buffer_load_b128(rk, voff, (80 + it * 16 + i) * 4096, AUX_NT);
          qk(kc, a0, c0);
          tree(a0, c0, (2 * i) & 15, pw);
          tree(a1, c1, (2 * i + 1) & 15, dst + (it * 2 + (i >> 3)) * 32);
          SCHED();
        }
      }
    }
#pragma unroll 1
    for (int it = 6; it < 8; ++it) {
      const rsrc_t rn = it < 7 ? rk : rv;
      const int sbase = it < 7 ? (it + 1) * (16 * 4096) : 0;
#pragma unroll
      for (int i = 0; i < 16; ++i) {
        float a, c;
        qk(ring[i], a, c);
        asm volatile("" : "+v"(a), "+v"(c));
        THROTTLE(i);
        PACE_WAIT();
        ring[i] = __builtin_amdgcn_raw_buffer_load_b128(rn, voff, sbase + i * 4096, AUX_NT);
        tree(a, c, i, pw + it * 64 + pidx);
        SCHED();
      }
    }
    mx = x32_max(row_max(mx)) * C2;
    float m0 = mx, m1 = mx;
    swap16(m0, m1);
    lds_fence();
    f32x4 sa = *(LAS f32x4*)(pw + lane * 8), sb = *(LAS f32x4*)(pw + lane * 8 + 4);
#pragma unroll
    for (int e = 0; e < 4; ++e) {
      sa[e] = __builtin_amdgcn_exp2f(fmaf(sa[e], C2, -((e & 1) ? m1 : m0)));
      sb[e] = __builtin_amdgcn_exp2f(fmaf(sb[e], C2, -((e & 1) ? m1 : m0)));
    }
    if (lane < 56) { *(LAS f32x4*)(pw + lane * 8) = sa; *(LAS f32x4*)(pw + lane * 8 + 4) = sb; }
    const float l0 = wave_sum((sa[0] + sa[2]) + (sb[0] + sb[2])), l1s = wave_sum((sa[1] + sa[3]) + (sb[1] + sb[3]));
    if (lane == 0) *(LAS f32x4*)(L + L_ML + w * 16) = f32x4{m0, l0, m1, l1s};
    lds_signal(L, L_FLAGK, lane);
    f32x2 a0[4], a1[4];
#pragma unroll
    for (int e = 0; e < 4; ++e) { a0[e] = f32x2{0.f, 0.f}; a1[e] = f32x2{0.f, 0.f}; }
    auto pv = [&](const u32x4 vv, float p0, float p1) {
#pragma unroll
      for (int e = 0; e < 4; ++e) {
        const f32x2 v2 = {bf_lo(vv[e]), bf_hi(vv[e])};
        a0[e] = __builtin_elementwise_fma(f32x2{p0, p0}, v2, a0[e]);
        a1[e] = __builtin_elementwise_fma(f32x2{p1, p1}, v2, a1[e]);
      }
    };
    const rsrc_t rvb = mk_rsrc((const char*)p.v + (size_t)grp * 4 * CHUNK, 4 * CHUNK);
    const int voffb = w * CHUNK + (896 + (lane >> 3)) * 512 + j * 128 + (lane & 7) * 16;
    const LAS f32x4* pr = (const LAS f32x4*)(pw + hf * 4);
    f32x4 pq[2] = {pr[0], pr[2]};
#pragma unroll 1
    for (int it = 0; it < 6; ++it) {
#pragma unroll
      for (int i = 0; i < 16; ++i) {
        const f32x4 pc = pq[(i >> 1) & 1];
        pv(ring[i], pc[2 * (i & 1)], pc[2 * (i & 1) + 1]);
        asm volatile("" : "+v"(a0[3]), "+v"(a1[3]));
        THROTTLE(i);
        PACE_WAIT();
        ring[i] = __builtin_amdgcn_raw_buffer_load_b128(rv, voff, ((it + 1) * 16 + i) * 4096, AUX_NT);
        if (i & 1) pq[(i >> 1) & 1] = pr[(it * 8 + (i >> 1) + 2) * 2];
        SCHED();
      }
    }
    auto req_b = [&](int i0) {
      THROTTLE(0);
#pragma unroll
      for (int i = i0; i < i0 + 4; ++i) {
        PACE_WAIT();
        ring[16 + i] = __builtin_amdgcn_raw_buffer_load_b128(rvb, voffb, i * 4096, AUX_NT);
        SCHED();
      }
    };
#pragma unroll
    for (int i = 0; i < 8; ++i) {
      if (i == 4) req_b(0);
      if (i == 6) req_b(4);
      const f32x4 pc = pq[i & 1];
      pv(ring[2 * i], pc[0], pc[1]);
      pv(ring[2 * i + 1], pc[2], pc[3]);
      if (i < 6) pq[i & 1] = pr[(48 + i + 2) * 2];
      SCHED();
    }
    {
      f32x4 r0, r1;
#pragma unroll
      for (int e = 0; e < 8; ++e) {
        float u = a0[e >> 1][e & 1], v = a1[e >> 1][e & 1];
        swap32(u, v);
        const float t = u + v;
        if (e < 4) r0[e] = t; else r1[e - 4] = t;
      }
      *(LAS f32x4*)(L + L_MRG + w * 32768 + lane * 32) = r0;
      *(LAS f32x4*)(L + L_MRG + w * 32768 + lane * 32 + 16) = r1;
      lds_signal(L, L_FLAGV, lane);
    }
    lds_wait_ge<1>(L, L_FLAGP, 4u);
    f32x2 pb[16];
#pragma unroll
    for (int i = 0; i < 16; ++i) pb[i] = *(const LAS f32x2*)(L + L_PB + (w * 128 + 8 * i + (lane >> 3)) * 8);
#pragma unroll
    for (int e = 0; e < 4; ++e) { a0[e] = f32x2{0.f, 0.f}; a1[e] = f32x2{0.f, 0.f}; }
    SCHED();
    req_b(8);
#pragma unroll
    for (int i = 0; i < 4; ++i) { pv(ring[16 + i], pb[i][0], pb[i][1]); SCHED(); }
    req_b(12);
#pragma unroll
    for (int i = 4; i < 16; ++i) { pv(ring[16 + i], pb[i][0], pb[i][1]); SCHED(); }
    {
      float t8[8];
#pragma unroll
      for (int e = 0; e < 8; ++e) {
        float u = a0[e >> 1][e & 1], v = a1[e >> 1][e & 1];
        swap32(u, v);
        t8[e] = u + v;
      }
      f32x4 r;
#pragma unroll
      for (int e = 0; e < 4; ++e) {
        swap16(t8[e], t8[e + 4]);
        const float u = t8[e] + t8[e + 4];
        r[e] = u + dpp<XOR8>(u);
      }
      put_rows(L + L_AB + (4 + w) * 272, hf * 16 + 2 * (lane & 7) + (g4 & 1), r);
    }
    wg_barrier();
    const int fi = kvh * 4 + j;
    if (w == 0 && fi < 28) {
      const unsigned tag = __builtin_amdgcn_readfirstlane(*(LAS unsigned*)(L + L_TAG));
      const int gq = lane & 3, sq = lane >> 2;
      const int col = 16 * fi + 4 * gq + (g4 >> 1) + 2 * (g4 & 1);
      const f32x2 gx = *(const LAS f32x2*)(L + L_GATE + lane * 8);
      float gate = gx[0], xv = gx[1];
      const int off[2] = {WS_S2 + ((b * 32 + sq) * 112 + 4 * fi + gq) * 16, WS_S2 + ((b * 32 + 16 + sq) * 112 + 4 * fi + gq) * 16};
      asm volatile("" : "+v"(gate), "+v"(xv));
      wait_until((unsigned)__builtin_amdgcn_s_memrealtime() + S2T);
      const f32x4 s2 = poll_pipe(ws, off[0], off[1], tag);
      const float sv = rnd_bf16(red4(s2));
      const float o = xv + rnd_bf16(sv * gate);
      const rsrc_t rout = mk_rsrc(p.out, NB * DM * 2);
      __builtin_amdgcn_raw_buffer_store_b16((unsigned short)pack_bf16(o, o), rout, (c16 < 4) ? (b * DM + col) * 2 : OOB, 0, 0);
    } else if (w == 1 && blk == 0) {
      const unsigned tag = *(LAS unsigned*)(L + L_TAG);
      int off[4];
      u32x4 g[4];
#pragma unroll
      for (int i = 0; i < 4; ++i) off[i] = WS_ML + (lane * 4 + i) * 128;
      sweep<4, 0, 8>(ws, off, tag, g);
      __builtin_amdgcn_raw_buffer_store_b32(tag, ws, lane == 0 ? WS_EPOCH : OOB, 0, AUX_SC1);
    }
    return;
  }

  const int hw = w - 4;
  const unsigned t1 = (unsigned)__builtin_amdgcn_s_memrealtime() - K0;
  __builtin_amdgcn_s_setprio(3);
  unsigned c0 = 0, c1 = 0;
  if (b == 7 && jr >= 2) {
    unsigned long long pc;
    unsigned len;
    asm volatile("s_getpc_b64 %0\n.Lcw%=:\n\ts_mov_b32 %1, .Lfdk_tail-.Lcw%=" : "=s"(pc), "=s"(len));
    const rsrc_t rc = mk_rsrc((const void*)pc, len < 65536u ? (int)len : 0);
    c0 = __builtin_amdgcn_raw_buffer_load_b32(rc, ((hw * 2 + jr - 2) * 64 + lane) * 128, 0, 0);
    c1 = __builtin_amdgcn_raw_buffer_load_b32(mk_rsrc(p.out, hw == 3 && jr == 3 ? NB * DM * 2 : 0), (lane & 1) * 4096, 0, AUX_NT);
    SCHED();
  }
  const bool job01 = jr < 2 || (jr == 2 && gr < 56), job2 = jr == 3 && gr < 49;
  const int jrow = 64 * hw + 16 * g4 + 8 * sub;
  u32x4 wr[8];
  auto job_load = [&](rsrc_t r, int stride, int base, int nrow) {
    const int o0 = jrow < nrow ? jrow * stride + base + (c16 & 7) * 16 : OOB;
#pragma unroll
    for (int jj = 0; jj < 8; ++jj) {
      wr[jj] = __builtin_amdgcn_raw_buffer_load_b128(r, o0 + jj * stride, 0, AUX_NT);
      SCHED();
    }
    SCHED();
  };
  auto job_mma = [&](int row0, int nrow) -> f32x4 {
    lds_wait_ge<1>(L, L_FLAGA, 4u);
    u32x4 af = *(LAS u32x4*)(L + L_XN + (c16 & 7) * XN_STRIDE + (row0 + (jrow < nrow ? jrow : 0)) * 2);
    float res[8];
#pragma unroll
    for (int m = 0; m < 8; ++m) {
      const f32x4 d = mfma16(af, frag_col(wr, m), f32x4{0.f, 0.f, 0.f, 0.f});
      float u = d[0], v = d[2], u2 = d[1], v2 = d[3];
      swap32(u, v); swap32(u2, v2);
      const float t1 = u + dpp<XOR8>(v), t2 = v2 + dpp<XOR8>(u2);
      res[m] = sub ? t2 : t1;
    }
    const int bb = 4 * (g4 & 1) + sub + 2 * (lane >> 5);
    LAS float* d = (LAS float*)(L + L_RED) + hw * 512 + bb * 64 + 8 * (c16 & 7);
    *(LAS f32x4*)d = f32x4{res[0], res[1], res[2], res[3]};
    *(LAS f32x4*)(d + 4) = f32x4{res[4], res[5], res[6], res[7]};
    lds_signal(L, L_FLAGB, lane);
    f32x4 v = {0.f, 0.f, 0.f, 0.f};
    if (hw < 2) {
      lds_wait_ge<1>(L, L_FLAGB, 4u);
      const LAS f32x4* s = (const LAS f32x4*)(L + L_RED) + 64 * hw + lane;
      v = (s[0] + s[128]) + (s[256] + s[384]);
    }
    return v;
  };
  const int hcol = gr < 48 ? (gr >> 4) * 16 + (gr & 7) * 2 + ((gr >> 3) & 1) : gr;
  const int qh = (xr + b) & 7;

  const int xrow = 2 * hw + (lane >> 5), xl = lane & 31;
  u32x4 xa, xb;
  if (job01) {
    const rsrc_t rx = mk_rsrc(p.x, NB * DM * 2);
    const int xoff = xl < 28 ? xrow * (DM * 2) + xl * 32 : OOB;
    xa = __builtin_amdgcn_raw_buffer_load_b128(rx, xoff, 0, AUX_NT);
    xb = __builtin_amdgcn_raw_buffer_load_b128(rx, xoff, 16, AUX_NT);
    const rsrc_t rw = jr < 2 ? mk_rsrc(p.wq, DM * HD * 2) : mk_rsrc(p.w1, DM * DFF * 2);
    const int stride = jr < 2 ? HD * 2 : DFF * 2;
    const int o0 = jrow * stride + (jr < 2 ? (qh * 512 + 64 * b + 32 * jr) * 2 : hcol * 64) + (c16 & 7) * 8;
#pragma unroll
    for (int jj = 0; jj < 16; ++jj) {
      const u32x2 t2 = __builtin_amdgcn_raw_buffer_load_b64(rw, o0 + ((jj >> 3) * 256 + (jj & 7)) * stride, 0, AUX_NT);
      wr[jj & 7][2 * (jj >> 3)] = t2[0]; wr[jj & 7][2 * (jj >> 3) + 1] = t2[1];
      SCHED();
    }
  }
  if (job2) job_load(mk_rsrc(p.w2, DFF * DM * 2), DM * 2, (gr / 7) * 256 * (DM * 2) + (gr % 7) * 128, 256);
  wg_barrier();
  const unsigned t0 = __builtin_amdgcn_readfirstlane(*(LAS unsigned*)(L + L_TAG + 4));
  if (hw == 3) {
    unsigned ep;
    asm volatile("s_load_dword %0, %1, 0x0\n\ts_waitcnt lgkmcnt(0)" : "=s"(ep) : "s"(p.ws) : "memory");
    if (lane == 0) *(LAS unsigned*)(L + L_TAG) = ep + 1u;
    lds_signal(L, L_FLAGT, lane);
  }
  if (job01) {
    float xv[16];
#pragma unroll
    for (int i = 0; i < 4; ++i) { xv[2 * i] = bf_lo(xa[i]); xv[2 * i + 1] = bf_hi(xa[i]); xv[8 + 2 * i] = bf_lo(xb[i]); xv[9 + 2 * i] = bf_hi(xb[i]); }
    float s = 0.f;
#pragma unroll
    for (int i = 0; i < 4; ++i) s += (xv[4 * i] + xv[4 * i + 1]) + (xv[4 * i + 2] + xv[4 * i + 3]);
    const float mean = x16_sum(row_sum(s)) * (1.0f / DM);
    float ss0 = 0.f, ss1 = 0.f;
#pragma unroll
    for (int i = 0; i < 8; ++i) { const float d = xv[i] - mean, e = xv[8 + i] - mean; ss0 = fmaf(d, d, ss0); ss1 = fmaf(e, e, ss1); }
    const float ss = xl < 28 ? ss0 + ss1 : 0.f;
    const float var = rnd_bf16(x16_sum(row_sum(ss)) * (1.0f / DM));
    const float mb = rnd_bf16(mean);
    const float den = rnd_bf16(__builtin_amdgcn_sqrtf(rnd_bf16(var + 1e-5f)));
    const float rden = __builtin_amdgcn_rcpf(den);
    u32x4 o0, o1;
#pragma unroll
    for (int i = 0; i < 4; ++i) {
      const unsigned d0 = pack_bf16(xv[2 * i] - mb, xv[2 * i + 1] - mb), d1 = pack_bf16(xv[8 + 2 * i] - mb, xv[9 + 2 * i] - mb);
      o0[i] = pack_bf16(bf_lo(d0) * rden, bf_hi(d0) * rden);
      o1[i] = pack_bf16(bf_lo(d1) * rden, bf_hi(d1) * rden);
    }
    if (xl < 28) {
      *(LAS u32x4*)(L + L_XN + xrow * XN_STRIDE + xl * 32) = o0;
      *(LAS u32x4*)(L + L_XN + xrow * XN_STRIDE + xl * 32 + 16) = o1;
    }
    lds_signal(L, L_FLAGA, lane);
  }
  lds_wait_ge<1>(L, L_FLAGT, 1u);
  const unsigned tag = __builtin_amdgcn_readfirstlane(*(LAS unsigned*)(L + L_TAG));
  const int t = hw * 64 + lane;
  if (job01) {
    lds_wait_ge<1>(L, L_FLAGA, 4u);
    const LAS unsigned char* ap = L + L_XN + (c16 & 7) * XN_STRIDE + jrow * 2;
    const u32x4 af0 = *(const LAS u32x4*)ap, af1 = *(const LAS u32x4*)(ap + (hw < 3 ? 512 : 0));
    f32x4 res;
#pragma unroll
    for (int m = 0; m < 4; ++m) {
      f32x4 d = mfma16(af0, frag_col(wr, m), f32x4{0.f, 0.f, 0.f, 0.f});
      d = mfma16(af1, frag_col(wr, 4 + m), d);
      float u = d[0], v = d[2], u2 = d[1], v2 = d[3];
      swap32(u, v); swap32(u2, v2);
      const float t1_ = u + dpp<XOR8>(v), t2_ = v2 + dpp<XOR8>(u2);
      res[m] = sub ? t2_ : t1_;
    }
    const int bb = 4 * (g4 & 1) + sub + 2 * (lane >> 5);
    *((LAS f32x4*)(L + L_RED) + hw * 64 + bb * 8 + (c16 & 7)) = res;
    lds_signal(L, L_FLAGB, lane);
    if (hw == 0) {
      lds_wait_ge<1>(L, L_FLAGB, 4u);
      const LAS f32x4* sp = (const LAS f32x4*)(L + L_RED) + lane;
      f32x4 v = (sp[0] + sp[64]) + (sp[128] + sp[192]);
#pragma unroll
      for (int i = 0; i < 4; ++i) { v[i] = rnd_bf16(v[i]); if (jr == 2) v[i] = fmaxf(v[i], 0.f); }
      const int off = jr < 2 ? WS_QG + (((lane >> 3) * HKV + qh) * 128 + 16 * b + 8 * jr + (lane & 7)) * 16
                             : WS_HG + (((hcol >> 3) * NB + (lane >> 3)) * 64 + 8 * (hcol & 7) + (lane & 7)) * 16;
      st_gran4(ws, off, v, tag, jr < 2 ? (jr == 0 ? t1 : (unsigned)__builtin_amdgcn_s_memrealtime() - PNOM) >> 2 : 0u);
    }
  }
  if (hw >= 2) {
    const int gi = t - 128;
    const int off[1] = {WS_QG + (grp * 128 + gi) * 16};
    u32x4 g[1];
    wait_until(t0 + Q_T);
    sweep<1, 1, 16>(ws, off, tag, g);
    {
      const int d = (int)(signed char)(side8(g[0]) - (t0 >> 2));
      const int s0 = 2 * (int)wave_sum(sub ? 0.f : (float)d), s1 = 2 * (int)wave_sum(sub ? (float)d : 0.f);
      if (lane < 2) __hip_atomic_fetch_add((LAS int*)(L + L_SHIFT) + lane, lane ? s1 : s0, __ATOMIC_RELAXED, __HIP_MEMORY_SCOPE_WORKGROUP);
      if (c16 == 0) __hip_atomic_fetch_max((LAS int*)(L + L_SHIFT) + 2, d + 128, __ATOMIC_RELAXED, __HIP_MEMORY_SCOPE_WORKGROUP);
    }
    *(LAS u32x2*)(L + L_Q + gi * 8) = u32x2{perm_hi(g[0][0], g[0][1]), perm_hi(g[0][2], g[0][3])};
    lds_signal(L, L_FLAGQ, lane);
  }
  asm volatile("" ::"v"(c0), "v"(c1));
  if (job2) {
    const int rblk = gr / 7, cblk = gr % 7;
    wait_until(t0 + Q_T);
    const int off[2] = {WS_HG + (rblk * 512 + t) * 16, WS_HG + (rblk * 512 + 256 + t) * 16};
    u32x4 g[2];
    sweep<2, 2, 16>(ws, off, tag, g);
#pragma unroll
    for (int i = 0; i < 2; ++i)
      *(LAS u32x2*)(L + L_XN + (4 * i + hw) * XN_STRIDE + lane * 8) = u32x2{perm_hi(g[i][0], g[i][1]), perm_hi(g[i][2], g[i][3])};
    lds_signal(L, L_FLAGA, lane);
    const f32x4 v = job_mma(0, 256);
    if (hw < 2) st_gran4(ws, WS_GG + (((t >> 4) * 7 + rblk) * 112 + 16 * cblk + (t & 15)) * 16, v, tag);
  }

  u32x4 wp[4][8];
  {
    const rsrc_t ro = mk_rsrc(p.wo, HD * DM * 2);
    const int oo = 16 * hw + c16 < 56 ? (kvh * 512 + 64 * j + g4) * (DM * 2) + (16 * hw + c16) * 16 : OOB;
#pragma unroll
    for (int r = 0; r < 32; ++r) {
      if (r % 16 == 0) wait_until(t0 + WO_T0 + r * WO_DT + (jr * 2 + (xr & 1)) * 2 * WO_DT);
      asm volatile("s_waitcnt vmcnt(%0)" ::"n"(15) : "memory");
      wp[r >> 3][r & 7] = __builtin_amdgcn_raw_buffer_load_b128(ro, oo, ((r >> 4) * 256 + ((r >> 3) & 1) * 32 + (r & 7) * 4) * (DM * 2), AUX_NT);
      SCHED();
    }
#pragma unroll
    for (int ks = 0; ks < 4; ++ks) {
      u32x4 f[8];
#pragma unroll
      for (int m = 0; m < 8; ++m) f[m] = frag_col(wp[ks], m);
#pragma unroll
      for (int m = 0; m < 8; ++m) { wp[ks][m] = f[m]; asm volatile("" : "+v"(wp[ks][m])); }
    }
  }
  float fw[4];
  {
    lds_wait_ge<32>(L, L_FLAGK, 4u);
    float mw[4];
#pragma unroll
    for (int ww = 0; ww < 4; ++ww) mw[ww] = *(const LAS float*)(L + L_ML + ww * 16 + (hw >> 1) * 8);
    if (hw == 1) {
      const LAS float* ps = (const LAS float*)(L + L_P) + (lane & 3) * 512 + (56 + (lane >> 3)) * 8 + ((lane >> 2) & 1) * 2;
      const f32x2 k0 = *(const LAS f32x2*)ps, k1 = *(const LAS f32x2*)(ps + 4);
      st_gran4(ws, WS_SB + ((grp * 4 + j) * 64 + lane) * 16, f32x4{k0[0], k0[1], k1[0], k1[1]}, tag);
    } else if (hw == 0) {
      float m2[4], M2, L2 = 0.f;
      f32x2 ml2[4];
#pragma unroll
      for (int ww = 0; ww < 4; ++ww) { ml2[ww] = *(const LAS f32x2*)(L + L_ML + ww * 16 + (lane & 1) * 8); m2[ww] = ml2[ww][0]; }
      M2 = fmaxf(fmaxf(m2[0], m2[1]), fmaxf(m2[2], m2[3]));
#pragma unroll
      for (int ww = 0; ww < 4; ++ww) L2 = fmaf(__builtin_amdgcn_exp2f(m2[ww] - M2), ml2[ww][1], L2);
      st_gran2(ws, lane < 8 ? WS_ML + ((grp * 4 + j) * 8 + lane) * 16 : OOB, M2, L2, tag);
    }
    const int fi = kvh * 4 + j, gq = lane & 3, sq = lane >> 2;
    const bool fin = hw == 3 && fi < 28;
    wait_until(t0 + ML_T);
    const unsigned xx = __builtin_amdgcn_raw_buffer_load_b16(mk_rsrc(p.x, NB * DM * 2),
                                                             fin ? (b * DM + 16 * fi + 4 * gq + (g4 >> 1) + 2 * (g4 & 1)) * 2 : OOB, 0, AUX_NT);
    const int off[3] = {WS_SB + ((grp * 4 + hw) * 64 + lane) * 16, fin && sq < 7 ? WS_GG + ((b * 7 + sq) * 112 + 4 * fi + gq) * 16 : OOB,
                        WS_ML + ((grp * 4 + (lane & 3)) * 8 + ((lane >> 2) & 1)) * 16};
    u32x4 g[3];
    sweep<3, 2, 16>(ws, off, tag, g);
    if (fin) {
      const float gs = rnd_bf16(red4(val4(g[1])));
      *(LAS f32x2*)(L + L_GATE + lane * 8) = f32x2{rnd_bf16(gs / (1.0f + __expf(-gs))), bf_lo(xx)};
    }
    const float ms = asf(g[2][0]), ls = asf(g[2][2]);
    float M = fmaxf(ms, dpp<XOR1>(ms)); M = fmaxf(M, dpp<XOR2>(M));
    float Lt = __builtin_amdgcn_exp2f(ms - M) * ls; Lt += dpp<XOR1>(Lt); Lt += dpp<XOR2>(Lt);
    const float inv = 1.0f / Lt;
    const float M0 = asf(__builtin_amdgcn_readlane(asu(M), 0)), M1 = asf(__builtin_amdgcn_readlane(asu(M), 4));
    const float i0 = asf(__builtin_amdgcn_readlane(asu(inv), 0)), i1 = asf(__builtin_amdgcn_readlane(asu(inv), 4));
    const f32x4 sc = val4(g[0]);
    *(LAS f32x4*)(L + L_PB + (hw * 64 + lane) * 16) =
        f32x4{__builtin_amdgcn_exp2f(fmaf(sc[0], C2, -M0)) * i0, __builtin_amdgcn_exp2f(fmaf(sc[1], C2, -M1)) * i1,
              __builtin_amdgcn_exp2f(fmaf(sc[2], C2, -M0)) * i0, __builtin_amdgcn_exp2f(fmaf(sc[3], C2, -M1)) * i1};
    lds_signal(L, L_FLAGP, lane);
    const float Mh = (hw >> 1) ? M1 : M0, ih = (hw >> 1) ? i1 : i0;
#pragma unroll
    for (int ww = 0; ww < 4; ++ww) fw[ww] = __builtin_amdgcn_exp2f(mw[ww] - Mh) * ih;
    asm volatile("" : "+v"(fw[0]), "+v"(fw[1]), "+v"(fw[2]), "+v"(fw[3]));
  }
  {
    lds_wait_ge<1>(L, L_FLAGV, 4u);
    const int gi = 32 * hw + (lane & 31);
    f32x4 v = {0.f, 0.f, 0.f, 0.f};
#pragma unroll
    for (int ww = 0; ww < 4; ++ww) v += fw[ww] * *(const LAS f32x4*)(L + L_MRG + ww * 32768 + gi * 16);
    const bool own = ((gi >> 4) & 3) == j;
    st_gran4(ws, lane < 32 && !own ? WS_S1 + ((grp * 4 + j) * 128 + gi) * 16 : OOB, v, tag);
    if (own) put_rows(L + L_AB + j * 272, (gi >> 6) * 16 + (gi & 15), v);
    if (hw != j) {
      const int off1[1] = {lane < 32 ? WS_S1 + ((grp * 4 + hw) * 128 + (lane >> 4) * 64 + 16 * j + c16) * 16 : OOB};
      u32x4 g[1];
      wait_until((unsigned)__builtin_amdgcn_s_memrealtime() + S1T);
      sweep<1, 1, 1>(ws, off1, tag, g);
      if (lane < 32) put_rows(L + L_AB + hw * 272, lane, val4(g[0]));
    }
  }
  int aoff = L_AB + (c16 & 7) * 272 + 16 * g4;
  const int gi2 = (16 * hw + c16) * 2 + (g4 & 1);
  int s2off = (lane < 32 && gi2 < 112) ? WS_S2 + ((b * 32 + kvh * 4 + j) * 112 + gi2) * 16 : OOB;
  asm volatile("" : "+v"(aoff), "+v"(s2off));
  wg_barrier();
  {
    u32x4 af[4];
#pragma unroll
    for (int ks = 0; ks < 4; ++ks) af[ks] = *(const LAS u32x4*)(L + aoff + 64 * ks);
    SCHED();
    f32x4 acc[8];
#pragma unroll
    for (int m = 0; m < 8; ++m) acc[m] = mfma16(af[0], wp[0][m], f32x4{0.f, 0.f, 0.f, 0.f});
    SCHED();
#pragma unroll
    for (int ks = 1; ks < 3; ++ks) {
#pragma unroll
      for (int m = 0; m < 8; ++m) acc[m] = mfma16(af[ks], wp[ks][m], acc[m]);
      SCHED();
    }
#pragma unroll
    for (int i = 0; i < 4; ++i) { acc[i] = mfma16(af[3], wp[3][i], acc[i]); acc[4 + i] = mfma16(af[3], wp[3][4 + i], acc[4 + i]); }
    SCHED();
    f32x4 v;
#pragma unroll
    for (int i = 0; i < 4; ++i) {
      const f32x2 l2 = f32x2{acc[i][0], acc[i][1]} + f32x2{acc[i][2], acc[i][3]};
      const f32x2 h2 = f32x2{acc[4 + i][0], acc[4 + i][1]} + f32x2{acc[4 + i][2], acc[4 + i][3]};
      float lo = l2[0] + l2[1], hi = h2[0] + h2[1];
      swap16(lo, hi);
      v[i] = lo + hi;
    }
    st_gran4(ws, s2off, v, tag);
  }
}

}

extern "C" void kernel_launch(void* const* d_in, const int* in_sizes, int n_in, void* d_out, int out_size, void* d_ws,
                              size_t ws_size, hipStream_t stream) {
  (void)in_sizes; (void)n_in; (void)out_size; (void)ws_size;
  Params p;
  p.x = d_in[0]; p.k = d_in[1]; p.v = d_in[2]; p.wq = d_in[3]; p.wo = d_in[4]; p.w1 = d_in[6]; p.w2 = d_in[7];
  p.out = d_out; p.ws = d_ws;
  (void)hipFuncSetAttribute((const void*)fused_decode_kernel, hipFuncAttributeMaxDynamicSharedMemorySize, LDS_BYTES);
  hipLaunchKernelGGL(fused_decode_kernel, dim3(NWG), dim3(NTHR), LDS_BYTES, stream, p);
}
```
